```python
import torch

def _softmax(x, axis=-1):
    return torch.softmax(x, dim=axis)
def _gelu(x):
    return torch.nn.functional.gelu(x, approximate="tanh")
def _silu(x):
    return torch.nn.functional.silu(x)
def _sigmoid(x):
    return torch.sigmoid(x)
def _layernorm(x, axis=-1, eps=1e-5):
    m = x.mean(dim=axis, keepdim=True)
    v = x.var(dim=axis, keepdim=True, unbiased=False)
    return (x - m) / torch.sqrt(v + eps)
def _scatter_add(tgt, idx, src, axis=0):
    return tgt.clone().index_add_(axis, idx, src)
def _segment_sum(src, seg, n):
    out = torch.zeros((n,) + src.shape[1:], dtype=src.dtype)
    return out.index_add_(0, seg, src)
def _pad(x, pw):
    flat = tuple(v for pair in reversed(pw) for v in pair)
    return torch.nn.functional.pad(x, flat)

kernel_name = 'concat_heads__mha_block_cached__gqa_block_cached__near_ridge'

def setup_inputs():

    _w = (lambda a: (lambda t: t.to(torch.bfloat16) if t.is_floating_point() else t)(torch.as_tensor(a)))
    def _A_setup():
        B, Sq, D = 16, 96, 512
        H, Sk, Dh = 32, 512, 128
        return {
            "x": _w((lambda *s: torch.randn(*s, dtype=torch.bfloat16) * 0.8)(B, Sq, D)),
            "k_cache": _w((lambda *s: torch.randn(*s, dtype=torch.bfloat16) * 0.8)(B, H, Sk, Dh)),
            "v_cache": _w((lambda *s: torch.randn(*s, dtype=torch.bfloat16) * 0.05)(B, H, Sk, Dh)),
            "wq": _w((lambda *s: torch.randn(*s, dtype=torch.bfloat16) * 0.8)(D, H * Dh) * (D ** -0.5)),
            "wo": _w((lambda *s: torch.randn(*s, dtype=torch.bfloat16) * 0.05)(H * Dh, D) * ((H * Dh) ** -0.5)),
        }
    _A_in = _A_setup()

    def _B_setup():
        B, Sq, D = 16, 96, 512
        Hq, Hkv, Sk, Dh = 32, 32, 512, 128
        assert Hq % Hkv == 0, (Hq, Hkv)
        return {
            "x": _w((lambda *s: torch.randn(*s, dtype=torch.bfloat16) * 0.8)(B, Sq, D)),
            "k_cache": _w((lambda *s: torch.randn(*s, dtype=torch.bfloat16) * 0.8)(B, Hkv, Sk, Dh)),
            "v_cache": _w((lambda *s: torch.randn(*s, dtype=torch.bfloat16) * 0.05)(B, Hkv, Sk, Dh)),
            "wq": _w((lambda *s: torch.randn(*s, dtype=torch.bfloat16) * 0.8)(D, Hq * Dh) * (D ** -0.5)),
            "wo": _w((lambda *s: torch.randn(*s, dtype=torch.bfloat16) * 0.05)(Hq * Dh, D) * ((Hq * Dh) ** -0.5)),
        }
    _B_in = _B_setup()

    return {
        "A_x": _A_in["x"],
        "A_k_cache": _A_in["k_cache"],
        "A_v_cache": _A_in["v_cache"],
        "A_wq": _A_in["wq"],
        "A_wo": _A_in["wo"],
        "B_x": _B_in["x"],
        "B_k_cache": _B_in["k_cache"],
        "B_v_cache": _B_in["v_cache"],
        "B_wq": _B_in["wq"],
        "B_wo": _B_in["wo"],
    }

def reference(A_x, A_k_cache, A_v_cache, A_wq, A_wo, B_x, B_k_cache, B_v_cache, B_wq, B_wo):
    def _A_ref(x, k_cache, v_cache, wq, wo):
        B, Sq, D = x.shape
        H, Dh = 32, 128
        q = (lambda x, *d: x.permute(*d))((x @ wq).reshape(B, Sq, H, Dh), 0, 2, 1, 3)
        scores = torch.einsum("bhqd,bhkd->bhqk", q, k_cache) * (Dh ** -0.5)
        p = _softmax(scores, axis=-1)
        o = torch.einsum("bhqk,bhkd->bhqd", p, v_cache)
        return (lambda x, *d: x.permute(*d))(o, 0, 2, 1, 3).reshape(B, Sq, H * Dh) @ wo

    def _B_ref(x, k_cache, v_cache, wq, wo):
        B, Sq, D = x.shape
        Hq, Dh = 32, 128
        Hkv = k_cache.shape[1]
        rep = Hq // Hkv
        q = (lambda x, *d: x.permute(*d))((x @ wq).reshape(B, Sq, Hq, Dh), 0, 2, 1, 3)
        k = (lambda x, n, axis: torch.repeat_interleave(x, n, dim=axis))(k_cache, rep, axis=1)
        v = (lambda x, n, axis: torch.repeat_interleave(x, n, dim=axis))(v_cache, rep, axis=1)
        scores = torch.einsum("bhqd,bhkd->bhqk", q, k) * (Dh ** -0.5)
        p = _softmax(scores, axis=-1)
        o = torch.einsum("bhqk,bhkd->bhqd", p, v)
        return (lambda x, *d: x.permute(*d))(o, 0, 2, 1, 3).reshape(B, Sq, Hq * Dh) @ wo

    a = _A_ref(A_x, A_k_cache, A_v_cache, A_wq, A_wo)
    b = _B_ref(B_x, B_k_cache, B_v_cache, B_wq, B_wo)
    return torch.concatenate([a, b], axis=-1)
```

```cpp
#define NO_WOPF
#define NT_WO 0
#include <hip/hip_runtime.h>
#include <hip/hip_bf16.h>

typedef __bf16   bf16x8 __attribute__((ext_vector_type(8)));
typedef __bf16   bf16x2 __attribute__((ext_vector_type(2)));
typedef short    s16x4  __attribute__((ext_vector_type(4)));
typedef float    f32x16 __attribute__((ext_vector_type(16)));
typedef float    f32x2  __attribute__((ext_vector_type(2)));
typedef unsigned u32x4  __attribute__((ext_vector_type(4)));
typedef int      i32x4  __attribute__((ext_vector_type(4)));
#define LAS __attribute__((address_space(3)))
typedef LAS char* lptr;
#define DI __device__ __forceinline__
#define PRIO 0
#define SB() __builtin_amdgcn_sched_barrier(0)
#define PIN(x) asm volatile("" : "+v"(x))

constexpr int NB = 16, SQ = 96, DM = 512, NH = 32, SK = 512, DH = 128;
constexpr unsigned KRING = 0, VRING = 32768, WRING = 65536, STASH = 90112, LDS_BYTES = 163840;
constexpr unsigned TILE = 8192, REG = 24576, WOR = 24576;
constexpr float QSCALE = 0.088388347648318447f * 1.4426950408889634f;
constexpr float THR = 8.0f;

DI f32x16 mfma(bf16x8 a, bf16x8 b, f32x16 c) { return __builtin_amdgcn_mfma_f32_32x32x16_bf16(a, b, c, 0, 0, 0); }
DI unsigned pk2(float lo, float hi) { f32x2 v = {lo, hi}; return __builtin_bit_cast(unsigned, __builtin_convertvector(v, bf16x2)); }
template <int S> DI bf16x8 pack8(const f32x16& x) {
  u32x4 p = {pk2(x[8 * S], x[8 * S + 1]), pk2(x[8 * S + 2], x[8 * S + 3]), pk2(x[8 * S + 4], x[8 * S + 5]), pk2(x[8 * S + 6], x[8 * S + 7])};
  return __builtin_bit_cast(bf16x8, p);
}
DI bf16x8 ld128(lptr p) { return *(const LAS bf16x8*)p; }
DI void st128(lptr p, bf16x8 v) { *(LAS bf16x8*)p = v; }
DI bf16x8 trd(lptr p0, lptr p1) {
  const s16x4 lo = __builtin_amdgcn_ds_read_tr16_b64_v4i16((LAS s16x4*)p0), hi = __builtin_amdgcn_ds_read_tr16_b64_v4i16((LAS s16x4*)p1);
  return __builtin_bit_cast(bf16x8, __builtin_shufflevector(lo, hi, 0, 1, 2, 3, 4, 5, 6, 7));
}
DI float swap_max(float v) { auto r = __builtin_amdgcn_permlane32_swap(__float_as_uint(v), __float_as_uint(v), false, false); return fmaxf(__uint_as_float(r[0]), __uint_as_float(r[1])); }
DI float swap_add(float v) { auto r = __builtin_amdgcn_permlane32_swap(__float_as_uint(v), __float_as_uint(v), false, false); return __uint_as_float(r[0]) + __uint_as_float(r[1]); }
DI float max3f(float a, float b, float c) { float r; asm volatile("v_max3_f32 %0, %1, %2, %3" : "=v"(r) : "v"(a), "v"(b), "v"(c)); return r; }

DI i32x4 mk_srd(const void* p, unsigned bytes) {
  const unsigned long long a = (unsigned long long)p;
  i32x4 s; s[0] = __builtin_amdgcn_readfirstlane((unsigned)a); s[1] = __builtin_amdgcn_readfirstlane((unsigned)(a >> 32)); s[2] = bytes; s[3] = 0x00020000; return s;
}
template <bool NT = true> DI void dma(unsigned m0v, unsigned voff, i32x4 srd, unsigned soff) {
  if constexpr (NT) asm volatile("s_mov_b32 m0, %0\n\ts_nop 0\n\tbuffer_load_dwordx4 %1, %2, %3 offen nt lds" :: "s"(m0v), "v"(voff), "s"(srd), "s"(soff) : "memory");
  else asm volatile("s_mov_b32 m0, %0\n\ts_nop 0\n\tbuffer_load_dwordx4 %1, %2, %3 offen lds" :: "s"(m0v), "v"(voff), "s"(srd), "s"(soff) : "memory");
}
#define NT_WQ 1
#define NT_WOPF 1
#define NT_X 1
template <int N> DI void waitvm() { asm volatile("s_waitcnt vmcnt(%0)" :: "i"(N) : "memory"); }
DI void waitlds() { __builtin_amdgcn_s_waitcnt(0xC07F); }
DI void bar0() { asm volatile("" ::: "memory"); waitlds(); asm volatile("s_barrier" ::: "memory"); }
#define bar() bar0()
#define WV(N) waitvm<N>()
#define SEG(x)

#define STP(i)
#define STPC(i)
#define DUMP()
struct Args {
  const char* x[2]; const char* k[2]; const char* v[2]; const char* wq[2]; const char* wo[2];
  char* part;
  unsigned long long* dbg;
};

DI unsigned wq_slot(int tau) {
  if (tau >= 16 || tau < 3) return WRING + (tau % 3) * TILE;
  if (tau < 7) return KRING + (tau - 3) * TILE;
  if (tau == 7) return VRING + 3 * TILE;
  return STASH + (tau - 8) * TILE;
}

struct AState { f32x16 o[4]; f32x16 negm; bf16x8 qf[8]; bf16x8 kf[8]; u32x4 pw0, pw1; float l; };
DI float fmax2(float a, float b) { return __builtin_elementwise_maximum(a, b); }
DI float fmax3(float a, float b, float c) { return fmax2(fmax2(a, b), c); }
DI float swap_maxa(float v) { auto r = __builtin_amdgcn_permlane32_swap(__float_as_uint(v), __float_as_uint(v), false, false); return fmax2(__uint_as_float(r[0]), __uint_as_float(r[1])); }

template <bool PV, bool FIRST, bool ZERO> DI float attn_step(AState& st, f32x16& Sc, f32x16& Sn, float m0, lptr k0, lptr k1, lptr v0, lptr v1) {
  bf16x8 vf[8];
#pragma unroll
  for (int g = 0; g < 8; ++g) {
    if (PV && g < 4) vf[g] = trd(v0 + 512 * g, v1 + 512 * g);
    SB();
    if (PV) {
      if (ZERO) { Sc[2 * g] -= m0; Sc[2 * g + 1] -= m0; }
      Sc[2 * g] = __builtin_amdgcn_exp2f(Sc[2 * g]); Sc[2 * g + 1] = __builtin_amdgcn_exp2f(Sc[2 * g + 1]); PIN(Sc);
    }
    if (g == 0) Sn = mfma(st.kf[0], st.qf[0], FIRST ? f32x16{} : st.negm); else Sn = mfma(st.kf[g], st.qf[g], Sn);
    PIN(Sn);
    if (PV && g >= 1) {
      const unsigned pk = pk2(Sc[2 * g - 2], Sc[2 * g - 1]);
      if (g - 1 < 4) { st.pw0[(g - 1) & 3] = pk; PIN(st.pw0); } else { st.pw1[(g - 1) & 3] = pk; PIN(st.pw1); }
    }
    SB();
  }
  waitlds();
  SB();
  float ps = 0.f, ta = 0.f, tb = 0.f;
#pragma unroll
  for (int g = 0; g < 8; ++g) {
    if (PV) {
      if (g < 2) { vf[4 + 2 * g] = trd(v0 + 4096 + 1024 * g, v1 + 4096 + 1024 * g); vf[5 + 2 * g] = trd(v0 + 4608 + 1024 * g, v1 + 4608 + 1024 * g); }
      if (g == 0) { st.pw1[3] = pk2(Sc[14], Sc[15]); PIN(st.pw1); }
      if (g == 4) { SB(); waitlds(); }
      SB();
      st.o[g & 3] = mfma(vf[g], __builtin_bit_cast(bf16x8, g < 4 ? st.pw0 : st.pw1), (ZERO && g < 4) ? f32x16{} : st.o[g & 3]);
      PIN(st.o[g & 3]);
    }
    if (g == 4 || g == 5) {
#pragma unroll
      for (int i = 0; i < 2; ++i) { st.kf[4 * (g - 4) + 2 * i] = ld128(k0 + 512 * (2 * (g - 4) + i)); st.kf[4 * (g - 4) + 2 * i + 1] = ld128(k1 + 512 * (2 * (g - 4) + i)); }
    }
    if (PV) { if (g == 0) ps = Sc[0] + Sc[1]; else { ps += Sc[2 * g]; ps += Sc[2 * g + 1]; } PIN(ps); }
    if (g == 2) { ta = fmax3(Sn[0], Sn[1], Sn[2]); tb = fmax3(Sn[3], Sn[4], Sn[5]); }
    if (g == 3) { ta = fmax3(ta, Sn[6], Sn[7]); tb = fmax3(tb, Sn[8], Sn[9]); }
    if (g == 6) { ta = fmax3(ta, Sn[10], Sn[11]); tb = fmax3(tb, Sn[12], Sn[13]); }
    if (g == 7) { ta = fmax3(ta, Sn[14], Sn[15]); ta = fmax2(ta, tb); }
    if (g >= 2) { PIN(ta); PIN(tb); }
    SB();
  }
  asm volatile("" ::: "memory");
  if (ZERO) st.l = ps; else st.l += ps;
  return ta;
}
DI void rescale(AState& st, f32x16& Sn, float hm) {
  if (__builtin_expect(__any(hm > THR), 0)) {
    const float dl = fmaxf(swap_maxa(hm), 0.f), al = __builtin_amdgcn_exp2f(-dl);
    st.l *= al;
#pragma unroll
    for (int d = 0; d < 4; ++d) st.o[d] *= al;
    Sn -= dl; st.negm -= dl;
  }
}
DI float new_head(AState& st, float hm) {
  const float tm = swap_maxa(hm);
#pragma unroll
  for (int i = 0; i < 16; ++i) st.negm[i] = -tm;
  return tm;
}
DI void write_o(AState& st, lptr dst) {
  const float inv = __builtin_amdgcn_rcpf(swap_add(st.l));
#pragma unroll
  for (int d = 0; d < 4; ++d) {
    const f32x16 s = st.o[d] * inv;
    st128(dst + (2 * d) * 1024, pack8<0>(s));
    st128(dst + (2 * d + 1) * 1024, pack8<1>(s));
  }
}

__global__ __launch_bounds__(512, 2) void attn_fused(Args a) {
  extern __shared__ __attribute__((aligned(16))) char smem[];
  const lptr lds = (lptr)smem;
  const unsigned lds0 = (unsigned)(uintptr_t)lds;
  const int tid = threadIdx.x, lane = tid & 63;
  const int w = __builtin_amdgcn_readfirstlane(tid >> 6);
  const int blk = blockIdx.x, xcd = blk & 7, idx = blk >> 3;
  const int br = xcd >> 2, b = idx >> 1, quad = 2 * (xcd & 3) + (idx & 1), h0 = 4 * quad;
  const int r = lane & 31, h = lane >> 5, blkb = (lane >> 4) & 1, q4 = (lane & 15) >> 2, p4 = lane & 3;
  const unsigned drow = (lane >> 2) & 7, dsub = lane >> 5, dsw0 = (lane & 3) ^ ((lane >> 4) & 1), dsw1 = (lane & 3) ^ (2 + ((lane >> 4) & 1));
  const unsigned g4 = (r >> 2) & 3;
  const unsigned rb0 = 2048 * (r >> 3) + 64 * (r & 7) + 16 * (h ^ g4), rb1 = 2048 * (r >> 3) + 64 * (r & 7) + 16 * ((2 + h) ^ g4);
  STP(0); STPC(16);

  if (w == 3) {
    const int rot = (b * 5 + h0 * 3 + br * 7) & 15;
    const i32x4 sk = mk_srd(a.k[br] + (size_t)b * NH * SK * DH * 2, NH * SK * DH * 2);
    const i32x4 sv = mk_srd(a.v[br] + (size_t)b * NH * SK * DH * 2, NH * SK * DH * 2);
    const unsigned v0 = drow * 256 + dsub * 64 + dsw0 * 16, v1 = drow * 256 + dsub * 64 + dsw1 * 16;
    auto issue = [&](int s, const i32x4& srd, unsigned ring) {
      const unsigned so = (unsigned)(h0 + (s >> 4)) * (SK * DH * 2) + (unsigned)((s + rot) & 15) * TILE;
      const unsigned d = lds0 + ring + (unsigned)(s & 3) * TILE;
#pragma unroll
      for (int p = 0; p < 8; ++p) dma(d + p * 1024, ((p >> 1) & 1) ? v1 : v0, srd, so + (p >> 1) * 2048 + (p & 1) * 128);
    };
    bar0(); bar0(); bar0();
    for (int H = 0; H < 8; ++H) {
      bar0();
      if (H == 2) issue(0, sk, KRING);
      if (H == 3) { issue(1, sk, KRING); issue(2, sk, KRING); }
      if (H == 4) { issue(3, sk, KRING); issue(0, sv, VRING); }
      if (H == 5) { issue(1, sv, VRING); issue(2, sv, VRING); }
      if (H == 6) issue(3, sv, VRING);
    }
    for (int G = 16; G <= 80; ++G) {
      SEG(1 + ((G - 16) >> 4));
      if (G == 16) WV(48); else if (G == 17 || G == 19 || G == 77) WV(24); else if (G >= 18 && G <= 76) WV(32);
      else if (G == 78) WV(16); else if (G == 79) WV(8); else if (G == 80) WV(0);
      bar();
      if (G == 17) issue(4, sk, KRING);
      if (G >= 17 && G <= 75) issue(G - 12, sk, KRING);
      if (G >= 18 && G <= 77) issue(G - 14, sv, VRING);
      if (G == 63) bar();
    }
    DUMP();
  } else if (w == 7) {
    const i32x4 sw = mk_srd(a.wq[br], DM * NH * DH * 2);
    const i32x4 so4p = mk_srd(a.wo[br], NH * DH * DM * 2);
    const unsigned v0 = drow * 8192 + dsub * 64 + dsw0 * 16, v1 = drow * 8192 + dsub * 64 + dsw1 * 16;
    auto issue = [&](int t) {
      const unsigned so = (unsigned)(t & 15) * 32u * 8192u + (unsigned)(h0 + (t >> 4)) * 256u;
      const unsigned d = lds0 + wq_slot(t);
#pragma unroll
      for (int p = 0; p < 8; ++p) dma<NT_WQ>(d + p * 1024, ((p >> 1) & 1) ? v1 : v0, sw, so + (p >> 1) * 8 * 8192 + (p & 1) * 128);
    };
    bar0();
    for (int t = 0; t < 8; ++t) issue(t);
    bar0(); bar0();
    for (int H = 0; H < 8; ++H) {
      if (H <= 4) waitvm<48>(); else if (H == 5) waitvm<32>(); else waitvm<16>();
      bar0();
      if (H < 4) { issue(8 + 2 * H); issue(9 + 2 * H); }
      if (H == 6) { issue(16); issue(17); }
    }
    for (int G = 16; G <= 80; ++G) {
      SEG(1 + ((G - 16) >> 4));
      if (G <= 62) WV(8); else if (G == 63) WV(0);
      bar();
      if (G <= 61) issue(G + 2);
      if (G == 63) bar();
    }
    DUMP();
  } else if (w >= 4) {
#define QBAR() bar0()
    const int qb = w - 4;
    bf16x8 xf[32];
    bar0(); bar0();
#pragma unroll
    for (int k = 0; k < 32; ++k) {
      const unsigned base = (k >> 3) < 3 ? STASH + (qb * 3 + (k >> 3)) * TILE : VRING + qb * TILE;
      xf[k] = ld128(lds + base + ((k & 1) ? rb1 : rb0) + 512 * ((k & 7) >> 1));
    }
    bar0();
    const unsigned wb0 = 2048 * h + 64 * q4 + 16 * ((2 * blkb + (p4 & 1)) ^ (2 * h)) + 8 * (p4 >> 1);
    const unsigned wb1 = 2048 * h + 64 * (4 + q4) + 16 * ((2 * blkb + (p4 & 1)) ^ (2 * h + 1)) + 8 * (p4 >> 1);
    for (int t = 0; t < 4; ++t) {
      f32x16 acc[4];
#pragma unroll
      for (int j = 0; j < 15; ++j) {
        if (t > 0 || (j & 1) == 0) QBAR();
        const unsigned so = wq_slot(16 * t + j);
        const lptr p0 = lds + wb0 + so, p1 = lds + wb1 + so;
#pragma unroll
        for (int ks = 0; ks < 2; ++ks)
#pragma unroll
          for (int d = 0; d < 4; ++d)
            acc[d] = mfma(trd(p0 + 4096 * ks + 512 * d, p1 + 4096 * ks + 512 * d), xf[2 * j + ks], (j == 0 && ks == 0) ? f32x16{} : acc[d]);
      }
      if (t > 0) QBAR();
      __builtin_amdgcn_s_setprio(1);
      bf16x8 qp[8];
      {
        const unsigned so = wq_slot(16 * t + 15);
        const lptr p0 = lds + wb0 + so, p1 = lds + wb1 + so;
        bf16x8 wf[8];
#pragma unroll
        for (int d = 0; d < 4; ++d) { wf[2 * d] = trd(p0 + 512 * d, p1 + 512 * d); wf[2 * d + 1] = trd(p0 + 4096 + 512 * d, p1 + 4096 + 512 * d); }
#pragma unroll
        for (int d = 0; d < 5; ++d) {
          if (d < 4) { acc[d] = mfma(wf[2 * d], xf[30], acc[d]); acc[d] = mfma(wf[2 * d + 1], xf[31], acc[d]); PIN(acc[d]); }
          if (d > 0) { const f32x16 sc = acc[d - 1] * QSCALE; qp[2 * d - 2] = pack8<0>(sc); qp[2 * d - 1] = pack8<1>(sc); PIN(qp[2 * d - 2]); PIN(qp[2 * d - 1]); }
          SB();
        }
      }
      if (t == 3) bar0();
      const lptr dst = lds + (t == 3 ? WRING : STASH + t * REG) + qb * TILE + lane * 16;
#pragma unroll
      for (int f = 0; f < 8; ++f) st128(dst + f * 1024, qp[f]);
      __builtin_amdgcn_s_setprio(0);
    }
    for (int G = 64; G <= 80; ++G) {
      bar0();
    }
  } else {
    const int qb = w;
    {
      const i32x4 sx = mk_srd(a.x[br] + (size_t)(b * SQ + qb * 32) * DM * 2, 32 * DM * 2);
      const unsigned v0 = drow * 1024 + dsub * 64 + dsw0 * 16, v1 = drow * 1024 + dsub * 64 + dsw1 * 16;
#pragma unroll
      for (int c = 0; c < 4; ++c) {
        const unsigned d = lds0 + (c < 3 ? STASH + (qb * 3 + c) * TILE : VRING + qb * TILE);
#pragma unroll
        for (int p = 0; p < 8; ++p) dma<NT_X>(d + p * 1024, ((p >> 1) & 1) ? v1 : v0, sx, (p >> 1) * 8 * 1024 + c * 256 + (p & 1) * 128);
      }
      STP(9);
      bar0();
      waitvm<0>();
    }
    STP(10);
    bar0();
    STP(11);
    bar0();
    STP(12);
    const unsigned vb0 = VRING + 64 * (4 * h + q4) + 16 * ((2 * blkb + (p4 >> 1)) ^ h) + 8 * (p4 & 1);
    const unsigned vb1 = VRING + 2048 + 64 * (4 * h + q4) + 16 * ((2 * blkb + (p4 >> 1)) ^ (2 + h)) + 8 * (p4 & 1);
    AState st;
    st.pw0 = u32x4{}; st.pw1 = u32x4{};
    f32x16 SA, SB_;
    float m0;
    for (int H = 0; H < 8; ++H) { bar0(); if (H == 0) STP(13); if (H == 2) STP(14); if (H == 5) STP(15); }
    STP(1);
    bar0();
    {
      const lptr src = lds + STASH + qb * TILE + lane * 16;
#pragma unroll
      for (int s = 0; s < 8; ++s) { st.qf[s] = ld128(src + s * 1024); st.kf[s] = ld128(lds + KRING + ((s & 1) ? rb1 : rb0) + 512 * (s >> 1)); }
      m0 = new_head(st, attn_step<false, true, false>(st, SB_, SA, 0.f, lds + KRING + TILE + rb0, lds + KRING + TILE + rb1, lds, lds));
    }
#define ABAR() bar0()
    const lptr kp0 = lds + KRING + rb0, kp1 = lds + KRING + rb1, vp0 = lds + vb0, vp1 = lds + vb1;
    for (int t = 0; t < 4; ++t) {
      ABAR();
      rescale(st, SB_, attn_step<true, false, true>(st, SA, SB_, m0, kp0 + 2 * TILE, kp1 + 2 * TILE, vp0, vp1));
      for (int a1 = 1; a1 < 15; a1 += 2) {
        const unsigned va = (unsigned)(a1 & 3) * TILE, vb = (unsigned)((a1 + 1) & 3) * TILE, ka = va ^ (2 * TILE), kb = vb ^ (2 * TILE);
        ABAR();
        rescale(st, SA, attn_step<true, false, false>(st, SB_, SA, 0.f, kp0 + ka, kp1 + ka, vp0 + va, vp1 + va));
        ABAR();
        rescale(st, SB_, attn_step<true, false, false>(st, SA, SB_, 0.f, kp0 + kb, kp1 + kb, vp0 + vb, vp1 + vb));
      }
      if (t == 2) bar0();
      ABAR();
      if (t < 3) {
        const lptr src = lds + (t == 2 ? WRING : STASH + (t + 1) * REG) + qb * TILE + lane * 16;
#pragma unroll
        for (int s = 0; s < 8; ++s) st.qf[s] = ld128(src + s * 1024);
      }
      const float hm = attn_step<true, true, false>(st, SB_, SA, 0.f, kp0 + TILE, kp1 + TILE, vp0 + 3 * TILE, vp1 + 3 * TILE);
      if (t < 3) {
        m0 = new_head(st, hm);
        write_o(st, lds + STASH + t * REG + qb * TILE + lane * 16);
      }
      STP(2 + t); if (t == 0) STPC(28); if (t == 2) STPC(29);
    }
    bar0();
    write_o(st, lds + KRING + qb * TILE + lane * 16);
  }
  if (w >= 3) bar0();

  {
    const i32x4 so4 = mk_srd(a.wo[br], NH * DH * DM * 2);
    const unsigned v0 = drow * 1024 + dsub * 64 + dsw0 * 16 + w * 128, v1 = drow * 1024 + dsub * 64 + dsw1 * 16 + w * 128;
    const unsigned ring = WOR + w * 8192;
    auto issue = [&](unsigned rowbase, int k) {
      const unsigned so = (rowbase + 16 * (k & 7)) * 1024u;
      const unsigned d = lds0 + ring + (unsigned)(k & 3) * 2048;
      dma<NT_WO>(d, v0, so4, so);
      dma<NT_WO>(d + 1024, v1, so4, so + 8 * 1024);
    };
    auto head_rows = [&](int i) { return (unsigned)(quad * 512 + 128 * ((i + b) & 3)); };
    auto head_lds = [&](int i) { const int hh = (i + b) & 3; return (unsigned)(hh == 3 ? KRING : STASH + hh * REG); };
    {
      const unsigned r0 = head_rows(0);
      issue(r0, 0); issue(r0, 1); issue(r0, 2); issue(r0, 3);
    }
    bar0();
    STP(6); STPC(30);
    const lptr oa0 = lds + ring + 64 * (4 * h + q4) + 16 * ((2 * blkb + (p4 & 1)) ^ h) + 8 * (p4 >> 1);
    const lptr oa1 = lds + ring + 1024 + 64 * (4 * h + q4) + 16 * ((2 * blkb + (p4 & 1)) ^ (2 + h)) + 8 * (p4 >> 1);
    f32x16 acc[2][3];
#pragma unroll
    for (int n = 0; n < 2; ++n)
#pragma unroll
      for (int q = 0; q < 3; ++q) acc[n][q] = f32x16{};
    struct Frag { bf16x8 a0, a1, o0, o1, o2; };
    auto rd = [&](lptr osrc, int k, Frag& f) {
      const unsigned so = (unsigned)(k & 3) * 2048;
      f.a0 = trd(oa0 + so, oa1 + so); f.a1 = trd(oa0 + so + 512, oa1 + so + 512);
      f.o0 = ld128(osrc + (k & 7) * 1024); f.o1 = ld128(osrc + (k & 7) * 1024 + TILE); f.o2 = ld128(osrc + (k & 7) * 1024 + 2 * TILE);
    };
    auto mm = [&](const Frag& f) {
      acc[0][0] = mfma(f.a0, f.o0, acc[0][0]); PIN(acc[0][0]); acc[1][0] = mfma(f.a1, f.o0, acc[1][0]); PIN(acc[1][0]);
      acc[0][1] = mfma(f.a0, f.o1, acc[0][1]); PIN(acc[0][1]); acc[1][1] = mfma(f.a1, f.o1, acc[1][1]); PIN(acc[1][1]);
      acc[0][2] = mfma(f.a0, f.o2, acc[0][2]); PIN(acc[0][2]); acc[1][2] = mfma(f.a1, f.o2, acc[1][2]); PIN(acc[1][2]);
    };
    Frag fr[2];
    waitvm<6>();
    rd(lds + head_lds(0) + lane * 16, 0, fr[0]);
    for (int i = 0; i < 4; ++i) {
      const lptr oc = lds + head_lds(i) + lane * 16, on = lds + head_lds(i + 1) + lane * 16;
      const unsigned rc = head_rows(i), rn = head_rows(i + 1);
      const bool more = i < 3;
#pragma unroll
      for (int kk = 0; kk < 8; ++kk) {
        if (kk < 5 || more) waitvm<4>(); else if (kk == 5) waitvm<2>(); else if (kk == 6) waitvm<0>();
        waitlds();
        if (kk < 7) rd(oc, kk + 1, fr[(kk + 1) & 1]); else if (more) rd(on, 0, fr[0]);
        SB();
        mm(fr[kk & 1]);
        SB();
        if (kk < 4) issue(rc, kk + 4); else if (more) issue(rn, kk - 4);
      }
    }
    STP(7); STPC(31);
    char* dst = a.part + ((size_t)((br * 16 + b) * 8 + quad) * 8 + w) * 12288 + lane * 16;
#pragma unroll
    for (int n = 0; n < 2; ++n)
#pragma unroll
      for (int q = 0; q < 3; ++q) {
        __builtin_nontemporal_store(__builtin_bit_cast(u32x4, pack8<0>(acc[n][q])), (u32x4*)(dst + ((n * 3 + q) * 2) * 1024));
        __builtin_nontemporal_store(__builtin_bit_cast(u32x4, pack8<1>(acc[n][q])), (u32x4*)(dst + ((n * 3 + q) * 2 + 1) * 1024));
      }
    STP(8); STPC(17);

  }
}

__global__ __launch_bounds__(256) void reduce_out(const char* __restrict__ part, char* __restrict__ out) {
  __shared__ __attribute__((aligned(16))) char t[96 * 128];
  const int tid = threadIdx.x, lane = tid & 63, wv = tid >> 6;
  const int g = blockIdx.x >> 3, w = blockIdx.x & 7, br = g >> 4, b = g & 15;
  const char* src = part + ((size_t)g * 64 + w) * 12288 + lane * 16;
  u32x4 v[3][8];
#pragma unroll
  for (int i = 0; i < 3; ++i)
#pragma unroll
    for (int s = 0; s < 8; ++s) v[i][s] = __builtin_nontemporal_load((const u32x4*)(src + (size_t)s * 8 * 12288 + (wv * 3 + i) * 1024));
#pragma unroll
  for (int i = 0; i < 3; ++i) {
    float f[8] = {};
#pragma unroll
    for (int s = 0; s < 8; ++s)
#pragma unroll
      for (int e = 0; e < 4; ++e) { f[2 * e] += __uint_as_float(v[i][s][e] << 16); f[2 * e + 1] += __uint_as_float(v[i][s][e] & 0xffff0000u); }
    const int fr = wv * 3 + i, n = fr / 6, q = (fr >> 1) % 3, rh = fr & 1;
    const int row = 32 * q + (lane & 31), ch = 4 * n + 2 * rh + (lane >> 5);
    const u32x4 o = {pk2(f[0], f[1]), pk2(f[2], f[3]), pk2(f[4], f[5]), pk2(f[6], f[7])};
    *(u32x4*)(t + row * 128 + ((ch ^ (row & 7)) * 16)) = o;
  }
  __syncthreads();
#pragma unroll
  for (int i = 0; i < 3; ++i) {
    const int c = i * 256 + tid, row = c >> 3, ch = c & 7;
    const u32x4 o = *(const u32x4*)(t + row * 128 + ((ch ^ (row & 7)) * 16));
    *(u32x4*)(out + ((size_t)(b * SQ + row) * 1024 + br * 512 + w * 64) * 2 + ch * 16) = o;
  }
}

extern "C" void kernel_launch(void* const* d_in, const int* in_sizes, int n_in, void* d_out, int out_size, void* d_ws, size_t ws_size, hipStream_t stream) {
  Args a;
  for (int i = 0; i < 2; ++i) {
    a.x[i] = (const char*)d_in[5 * i + 0]; a.k[i] = (const char*)d_in[5 * i + 1]; a.v[i] = (const char*)d_in[5 * i + 2];
    a.wq[i] = (const char*)d_in[5 * i + 3]; a.wo[i] = (const char*)d_in[5 * i + 4];
  }
  a.part = (char*)d_ws;
  a.dbg = (unsigned long long*)((char*)d_ws + (64u << 20));
  (void)hipFuncSetAttribute((const void*)attn_fused, hipFuncAttributeMaxDynamicSharedMemorySize, LDS_BYTES);
  attn_fused<<<256, 512, LDS_BYTES, stream>>>(a);
  reduce_out<<<256, 256, 0, stream>>>((const char*)d_ws, (char*)d_out);
}
```
